```python
import torch

def _softmax(x, axis=-1):
    return torch.softmax(x, dim=axis)
def _gelu(x):
    return torch.nn.functional.gelu(x, approximate="tanh")
def _silu(x):
    return torch.nn.functional.silu(x)
def _sigmoid(x):
    return torch.sigmoid(x)
def _layernorm(x, axis=-1, eps=1e-5):
    m = x.mean(dim=axis, keepdim=True)
    v = x.var(dim=axis, keepdim=True, unbiased=False)
    return (x - m) / torch.sqrt(v + eps)
def _scatter_add(tgt, idx, src, axis=0):
    return tgt.clone().index_add_(axis, idx, src)
def _segment_sum(src, seg, n):
    out = torch.zeros((n,) + src.shape[1:], dtype=src.dtype)
    return out.index_add_(0, seg, src)
def _pad(x, pw):
    flat = tuple(v for pair in reversed(pw) for v in pair)
    return torch.nn.functional.pad(x, flat)

kernel_name = 'recurrent_scan__ssm_conv_s4__mixer_token_mlp__prompt'

def setup_inputs():

    _w = (lambda a: (lambda t: t.to(torch.bfloat16) if t.is_floating_point() else t)(torch.as_tensor(a)))
    def _A_setup():
        B, L, D, N = 1, 2048, 512, 128
        A_re = -(lambda _x: torch.exp(torch.as_tensor(_x)))((lambda *s: torch.randn(*s, dtype=torch.bfloat16) * 3.2)(D, N))
        A_im = torch.pi * (lambda *s: torch.randn(*s, dtype=torch.bfloat16) * 0.2)(D, N)
        B_re = (lambda *s: torch.randn(*s, dtype=torch.bfloat16) * 0.2)(D, N)
        B_im = (lambda *s: torch.randn(*s, dtype=torch.bfloat16) * 0.2)(D, N)
        C_re = (lambda *s: torch.randn(*s, dtype=torch.bfloat16) * 0.2)(D, N)
        C_im = (lambda *s: torch.randn(*s, dtype=torch.bfloat16) * 0.2)(D, N)
        log_dt = (lambda *s: torch.randn(*s, dtype=torch.bfloat16) * 0.2)(D) * 0.5 - 4.0
        Dskip = (lambda *s: torch.randn(*s, dtype=torch.bfloat16) * 0.2)(D)
        return {
            "u": _w((lambda *s: torch.randn(*s, dtype=torch.bfloat16) * 0.05)(B, L, D)),
            "A_re": _w(A_re),
            "A_im": _w(A_im),
            "B_re": _w(B_re),
            "B_im": _w(B_im),
            "C_re": _w(C_re),
            "C_im": _w(C_im),
            "log_dt": _w(log_dt),
            "Dskip": _w(Dskip),
        }
    _A_in = _A_setup()

    def _B_setup():
        B, S, D, F = 1, 2048, 512, 1792
        return {
            "x": _w((lambda *s: torch.randn(*s, dtype=torch.bfloat16) * 0.05)(B, S, D)),
            "w1": _w((lambda *s: torch.randn(*s, dtype=torch.bfloat16) * 0.8)(S, F) * (S ** -0.5)),
            "b1": _w((lambda *s: torch.randn(*s, dtype=torch.bfloat16) * 12.8)(F) * 0.02),
            "w2": _w((lambda *s: torch.randn(*s, dtype=torch.bfloat16) * 0.2)(F, S) * (F ** -0.5)),
            "b2": _w((lambda *s: torch.randn(*s, dtype=torch.bfloat16) * 1.6)(S) * 0.02),
        }
    _B_in = _B_setup()

    return {
        "A_u": _A_in["u"],
        "A_A_re": _A_in["A_re"],
        "A_A_im": _A_in["A_im"],
        "A_B_re": _A_in["B_re"],
        "A_B_im": _A_in["B_im"],
        "A_C_re": _A_in["C_re"],
        "A_C_im": _A_in["C_im"],
        "A_log_dt": _A_in["log_dt"],
        "A_Dskip": _A_in["Dskip"],
        "B_x": _B_in["x"],
        "B_w1": _B_in["w1"],
        "B_b1": _B_in["b1"],
        "B_w2": _B_in["w2"],
        "B_b2": _B_in["b2"],
    }

def reference(A_u, A_A_re, A_A_im, A_B_re, A_B_im, A_C_re, A_C_im, A_log_dt, A_Dskip, B_x, B_w1, B_b1, B_w2, B_b2):
    def _A_ref(u, A_re, A_im, B_re, B_im, C_re, C_im, log_dt, Dskip):
        Bsz, L, D = u.shape
        dt = torch.exp(log_dt)[:, None]
        A = A_re + 1j * A_im
        Bc = B_re + 1j * B_im
        Cc = C_re + 1j * C_im
        logdA = dt * A
        dB = dt * Bc
        l = (lambda x, dt: x.to(dt))(torch.arange(L), A_re.dtype)
        P = torch.exp(logdA[:, :, None] * l[None, None, :])
        K = torch.einsum("dn,dnl,dn->dl", Cc, P, dB).real
        L2 = 2 * L
        Kp = _pad(K, ((0, 0), (0, L)))
        up = (lambda x, *d: x.permute(*d))(u, 0, 2, 1)
        up = _pad(up, ((0, 0), (0, 0), (0, L)))
        Kf = (lambda x, axes=(-2,-1): torch.fft.rfft2(x.float(), dim=axes))(Kp[:, None, :], axes=(-2, -1))
        Uf = (lambda x, axes=(-2,-1): torch.fft.rfft2(x.float(), dim=axes))(up[:, :, None, :], axes=(-2, -1))
        Yf = Uf * Kf[None, :, :, :]
        y = (lambda x, s, axes=(-2,-1): torch.fft.irfft2(x, s=s, dim=axes).to(torch.bfloat16))(Yf, (1, L2), axes=(-2, -1))[:, :, 0, :L]
        y = (lambda x, *d: x.permute(*d))(y, 0, 2, 1) + u * Dskip[None, None, :]
        return y

    def _B_ref(x, w1, b1, w2, b2):
        B, S, D = x.shape
        h = _layernorm(x, axis=-1)
        u = torch.einsum("bsd,sf->bdf", h, w1) + b1
        u = _gelu(u)
        y = torch.einsum("bdf,fs->bds", u, w2) + b2
        return (lambda x, *d: x.permute(*d))(y, 0, 2, 1)

    x = A_u
    xn = _layernorm(x, axis=-1)
    s = _A_ref(xn, A_A_re, A_A_im, A_B_re, A_B_im, A_C_re, A_C_im, A_log_dt, A_Dskip)
    g = _silu(_B_ref(xn, B_w1, B_b1, B_w2, B_b2))
    return x + s * g
```

```cpp
#include <hip/hip_runtime.h>
#include <stdint.h>

typedef unsigned short u16;
typedef __attribute__((ext_vector_type(8))) short bf16x8;
typedef __attribute__((ext_vector_type(4))) short s16x4;
typedef __attribute__((ext_vector_type(4))) float f32x4;
typedef __attribute__((ext_vector_type(2))) float f32x2;
typedef __attribute__((ext_vector_type(2))) __bf16 bf16x2;
typedef __attribute__((ext_vector_type(4))) unsigned u32x4;
typedef __attribute__((ext_vector_type(2))) unsigned u32x2;
#define LAS __attribute__((address_space(3)))

constexpr int L = 2048, D = 512, NS = 128, F = 1792;
constexpr int RS = 2112;

constexpr size_t WS_HT = 0;
constexpr size_t WS_XNT = WS_HT + (size_t)D * L * 2;
constexpr size_t WS_R = WS_XNT + (size_t)D * L * 2;
constexpr size_t WS_UT = WS_R + (size_t)D * RS * 2;
constexpr size_t WS_ST = WS_UT + (size_t)D * F * 2;

__device__ __forceinline__ float bf2f(u16 v) { return __uint_as_float(((unsigned)v) << 16); }
__device__ __forceinline__ float bflo(unsigned v) { return __uint_as_float(v << 16); }
__device__ __forceinline__ float bfhi(unsigned v) { return __uint_as_float(v & 0xffff0000u); }
__device__ __forceinline__ unsigned pk2(float a, float b) {
  f32x2 f = {a, b};
  return __builtin_bit_cast(unsigned, __builtin_convertvector(f, bf16x2));
}
__device__ __forceinline__ float rbf(float f) { return bflo(pk2(f, f)); }
__device__ __forceinline__ unsigned rfl(unsigned v) { return __builtin_amdgcn_readfirstlane(v); }

template <int CTRL>
__device__ __forceinline__ float dpp_add(float v) {
  int t = __builtin_amdgcn_update_dpp(0, __float_as_int(v), CTRL, 0xf, 0xf, true);
  return v + __int_as_float(t);
}
__device__ __forceinline__ float wave_sum(float v) {
  v = dpp_add<0xB1>(v);
  v = dpp_add<0x4E>(v);
  v = dpp_add<0x141>(v);
  v = dpp_add<0x140>(v);
  auto a = __builtin_amdgcn_permlane16_swap(__float_as_uint(v), __float_as_uint(v), false, false);
  v = __uint_as_float(a[0]) + __uint_as_float(a[1]);
  auto b = __builtin_amdgcn_permlane32_swap(__float_as_uint(v), __float_as_uint(v), false, false);
  return __uint_as_float(b[0]) + __uint_as_float(b[1]);
}
__device__ __forceinline__ s16x4 lds_tr(unsigned addr) {
  return __builtin_amdgcn_ds_read_tr16_b64_v4i16((LAS s16x4*)(size_t)addr);
}
__device__ __forceinline__ bf16x8 cat8(s16x4 a, s16x4 b) {
  return __builtin_shufflevector(a, b, 0, 1, 2, 3, 4, 5, 6, 7);
}

constexpr int LN_BLOCKS = 128;
constexpr int LN_ROWB = 1088;

__device__ __forceinline__ void cexp_pos(float ar2, float wh, float wl, float p, float& re, float& im) {
  float e = __builtin_amdgcn_exp2f(ar2 * p);
  float th = wh * p;
  float tl = __builtin_fmaf(wh, p, -th);
  tl = __builtin_fmaf(wl, p, tl);
  float r = __builtin_amdgcn_fractf(th) + tl;
  re = e * __builtin_amdgcn_cosf(r);
  im = e * __builtin_amdgcn_sinf(r);
}

__global__ __launch_bounds__(256) void k1_ln_kgen(const u16* __restrict__ x, const u16* __restrict__ Are,
                                                  const u16* __restrict__ Aim, const u16* __restrict__ Bre,
                                                  const u16* __restrict__ Bim, const u16* __restrict__ Cre,
                                                  const u16* __restrict__ Cim, const u16* __restrict__ logdt,
                                                  char* __restrict__ ws) {
  __shared__ __attribute__((aligned(16))) char smem[2 * 16 * LN_ROWB];
  const int tid = threadIdx.x, lane = tid & 63, wv = tid >> 6;
  const unsigned sbase = (unsigned)(size_t)(LAS char*)smem;
  if (blockIdx.x < LN_BLOCKS) {
    const int g = (blockIdx.x & 7) * (LN_BLOCKS / 8) + (blockIdx.x >> 3);
    const int t0 = g * 16;
    float v[4][8];
#pragma unroll
    for (int r = 0; r < 4; ++r) {
      u32x4 q = *(const u32x4*)(x + (size_t)(t0 + wv * 4 + r) * D + lane * 8);
#pragma unroll
      for (int e = 0; e < 4; ++e) { v[r][2 * e] = bflo(q[e]); v[r][2 * e + 1] = bfhi(q[e]); }
    }
#pragma unroll
    for (int pass = 0; pass < 2; ++pass) {
      float mean[4], var[4];
#pragma unroll
      for (int r = 0; r < 4; ++r) {
        float s = 0.f;
#pragma unroll
        for (int e = 0; e < 8; ++e) s += v[r][e];
        mean[r] = wave_sum(s) * (1.f / D);
      }
#pragma unroll
      for (int r = 0; r < 4; ++r) {
        float s = 0.f;
#pragma unroll
        for (int e = 0; e < 8; ++e) { float c = v[r][e] - mean[r]; s = __builtin_fmaf(c, c, s); }
        var[r] = wave_sum(s) * (1.f / D);
      }
#pragma unroll
      for (int r = 0; r < 4; ++r) {
        const float mb = rbf(mean[r]);
        const float den = rbf(__builtin_sqrtf(rbf(rbf(var[r]) + 1e-5f)));
        const float inv = 1.f / den;
        u32x4 o;
#pragma unroll
        for (int e = 0; e < 4; ++e) {
          unsigned c = pk2(v[r][2 * e] - mb, v[r][2 * e + 1] - mb);
          o[e] = pk2(bflo(c) * inv, bfhi(c) * inv);
          v[r][2 * e] = bflo(o[e]);
          v[r][2 * e + 1] = bfhi(o[e]);
        }
        *(u32x4*)(smem + pass * 16 * LN_ROWB + (wv * 4 + r) * LN_ROWB + lane * 16) = o;
      }
    }
    __syncthreads();
    const int i = lane & 15, c1 = (lane >> 4) & 1, q = lane >> 5, rr = i >> 2, p = lane & 3;
    const unsigned rowa = sbase + (8 * q + rr) * LN_ROWB;
    u16* xnT = (u16*)(ws + WS_XNT);
    char* hT = ws + WS_HT;
#pragma unroll
    for (int it = 0; it < 4; ++it) {
      const int m = wv * 4 + it;
      {
        unsigned a = rowa + (32 * m + 16 * c1 + 4 * p) * 2;
        bf16x8 val = cat8(lds_tr(a), lds_tr(a + 4 * LN_ROWB));
        *(bf16x8*)(xnT + (size_t)(32 * m + 16 * c1 + i) * L + t0 + 8 * q) = val;
      }
      {
        unsigned a = rowa + 16 * LN_ROWB + (32 * m + 8 * p + 4 * c1) * 2;
        bf16x8 val = cat8(lds_tr(a), lds_tr(a + 4 * LN_ROWB));
        const int dblk = m >> 1, dt = 2 * (m & 1) + c1, ks = g >> 1, qq = 2 * (g & 1) + q;
        *(bf16x8*)(hT + ((size_t)((dblk * 64 + ks) * 4 + dt)) * 1024 + (qq * 16 + i) * 16) = val;
      }
    }
    return;
  }
  const int d = blockIdx.x - LN_BLOCKS;
  f32x4* P4 = (f32x4*)smem;
  float* P1 = (float*)(smem + 2048);
  f32x4* red = (f32x4*)(smem + 4096);
  u16* Ktab = (u16*)(smem + 4096 + 12288);
  if (tid < NS) {
    const int idx = d * NS + tid;
    const float dt = rbf(expf(bf2f(logdt[d])));
    const float ar = dt * bf2f(Are[idx]), ai = dt * bf2f(Aim[idx]);
    const float br = dt * bf2f(Bre[idx]), bi = dt * bf2f(Bim[idx]);
    const float cr = bf2f(Cre[idx]), ci = bf2f(Cim[idx]);
    const float I2PH = 0.15915494f, I2PL = 6.4206382e-9f;
    const float wh = ai * I2PH;
    const float wl = __builtin_fmaf(ai, I2PL, __builtin_fmaf(ai, I2PH, -wh));
    f32x4 pp = {ar * 1.44269504089f, wh, wl, cr * br - ci * bi};
    P4[tid] = pp;
    P1[tid] = cr * bi + ci * br;
  }
  __syncthreads();
  const int i = lane & 15, q = lane >> 4;
  const float p0 = (float)((i & 7) + 128 * (i >> 3));
  const float p1 = (float)(i < 8 ? 256 + 128 * (i >> 2) + 2 * (i & 3) : 512 + 128 * ((i - 8) >> 1) + 4 * (i & 1));
  const float p2 = (float)(1024 + 128 * (i < 8 ? i : 8));
  const float py = (float)(8 * i);
  f32x4 acc0 = {0.f, 0.f, 0.f, 0.f}, acc1 = acc0, acc2 = acc0;
#pragma unroll
  for (int t = 0; t < 8; ++t) {
    const int n = 32 * wv + 4 * t + q;
    const f32x4 pp = P4[n];
    const float ci = P1[n];
    float yr, yi, xr, xi;
    cexp_pos(pp[0], pp[1], pp[2], py, yr, yi);
    const float gr = pp[3] * yr - ci * yi;
    const float gi = -(pp[3] * yi + ci * yr);
    cexp_pos(pp[0], pp[1], pp[2], p0, xr, xi);
    acc0 = __builtin_amdgcn_mfma_f32_16x16x4f32(xr, gr, acc0, 0, 0, 0);
    acc0 = __builtin_amdgcn_mfma_f32_16x16x4f32(xi, gi, acc0, 0, 0, 0);
    cexp_pos(pp[0], pp[1], pp[2], p1, xr, xi);
    acc1 = __builtin_amdgcn_mfma_f32_16x16x4f32(xr, gr, acc1, 0, 0, 0);
    acc1 = __builtin_amdgcn_mfma_f32_16x16x4f32(xi, gi, acc1, 0, 0, 0);
    cexp_pos(pp[0], pp[1], pp[2], p2, xr, xi);
    acc2 = __builtin_amdgcn_mfma_f32_16x16x4f32(xr, gr, acc2, 0, 0, 0);
    acc2 = __builtin_amdgcn_mfma_f32_16x16x4f32(xi, gi, acc2, 0, 0, 0);
  }
  red[(wv * 3 + 0) * 64 + lane] = acc0;
  red[(wv * 3 + 1) * 64 + lane] = acc1;
  red[(wv * 3 + 2) * 64 + lane] = acc2;
  __syncthreads();
  if (wv < 3) {
    f32x4 s = red[(0 * 3 + wv) * 64 + lane] + red[(1 * 3 + wv) * 64 + lane];
    s += red[(2 * 3 + wv) * 64 + lane] + red[(3 * 3 + wv) * 64 + lane];
#pragma unroll
    for (int r = 0; r < 4; ++r) {
      const int row = 4 * q + r;
      int pos;
      if (wv == 0) pos = (row & 7) + 128 * (row >> 3);
      else if (wv == 1) pos = row < 8 ? 256 + 128 * (row >> 2) + 2 * (row & 3) : 512 + 128 * ((row - 8) >> 1) + 4 * (row & 1);
      else pos = 1024 + 128 * (row < 8 ? row : 8);
      Ktab[pos + 8 * i] = (u16)pk2(s[r], s[r]);
    }
  }
  __syncthreads();
  {
    u16* Rrow = (u16*)(ws + WS_R) + (size_t)d * RS;
    u32x4 o;
#pragma unroll
    for (int e = 0; e < 4; ++e) {
      const int l0 = 2047 - 8 * tid - 2 * e;
      const unsigned rp = pk2((float)l0, (float)(l0 - 1));
      const int a = (int)bflo(rp), b = (int)bfhi(rp);
      o[e] = (unsigned)Ktab[a] | ((unsigned)Ktab[b] << 16);
    }
    *(u32x4*)(Rrow + 8 + 8 * tid) = o;
    if (tid < 8) {
      u32x4 z = {0u, 0u, 0u, 0u};
      *(u32x4*)(Rrow + (tid == 0 ? 0 : 8 * (256 + tid))) = z;
    }
  }
}

#define WAITV(n) asm volatile("s_waitcnt vmcnt(" #n ")" ::: "memory")

template <int N>
__device__ __forceinline__ void wait_stage(bf16x8& a0, bf16x8& a1, bf16x8& a2, bf16x8& a3) {
  asm volatile("s_waitcnt vmcnt(%4)" : "+v"(a0), "+v"(a1), "+v"(a2), "+v"(a3) : "n"(N) : "memory");
}

struct GemmCtx {
  __amdgpu_buffer_rsrc_t srdA, srdW;
  unsigned a_soff, a_step;
  unsigned w_soff, w_step;
  unsigned w_row8;
  unsigned vA, vW0, vW1;
  unsigned ring;
  unsigned tra[4];
};

__device__ __forceinline__ void issue_w(const GemmCtx& c, int t, int slot) {
  const unsigned so = c.w_soff + t * c.w_step;
  asm volatile(
      "s_waitcnt lgkmcnt(0)\n\t"
      "s_mov_b32 m0, %0\n\t"
      "s_nop 0\n\t"
      "buffer_load_dwordx4 %1, %3, %4 offen lds\n\t"
      "buffer_load_dwordx4 %2, %3, %5 offen offset:1024 lds\n\t"
      "buffer_load_dwordx4 %1, %3, %6 offen offset:2048 lds\n\t"
      "buffer_load_dwordx4 %2, %3, %7 offen offset:3072 lds"
      :
      : "s"(c.ring + slot * 4096), "v"(c.vW0), "v"(c.vW1), "s"(c.srdW), "s"(so), "s"(so + c.w_row8),
        "s"(so + 2 * c.w_row8), "s"(so + 3 * c.w_row8)
      : "memory");
}
__device__ __forceinline__ void issue_a(const GemmCtx& c, int t, bf16x8& a0, bf16x8& a1, bf16x8& a2, bf16x8& a3) {
  const unsigned so = c.a_soff + t * c.a_step;
  asm volatile(
      "buffer_load_dwordx4 %0, %4, %5, %6 offen\n\t"
      "buffer_load_dwordx4 %1, %4, %5, %6 offen offset:1024\n\t"
      "buffer_load_dwordx4 %2, %4, %5, %6 offen offset:2048\n\t"
      "buffer_load_dwordx4 %3, %4, %5, %6 offen offset:3072"
      : "=&v"(a0), "=&v"(a1), "=&v"(a2), "=&v"(a3)
      : "v"(c.vA), "s"(c.srdA), "s"(so)
      : "memory");
}

template <int T, int P, bool W_IS_A, int TR2>
__device__ __forceinline__ void gemm_core(const GemmCtx& c, f32x4 (&acc)[4][4]) {
  bf16x8 a[P][4];
#pragma unroll
  for (int s = 0; s < P; ++s) {
    issue_w(c, s, s);
    issue_a(c, s, a[s][0], a[s][1], a[s][2], a[s][3]);
  }
#pragma unroll
  for (int t = 0; t < T; ++t) {
    const int s = t % P;
    const int rem = (T - 1 - t) < (P - 1) ? (T - 1 - t) : (P - 1);
    switch (rem) {
      case 0: wait_stage<0>(a[s][0], a[s][1], a[s][2], a[s][3]); break;
      case 1: wait_stage<8>(a[s][0], a[s][1], a[s][2], a[s][3]); break;
      case 2: wait_stage<16>(a[s][0], a[s][1], a[s][2], a[s][3]); break;
      case 3: wait_stage<24>(a[s][0], a[s][1], a[s][2], a[s][3]); break;
      case 4: wait_stage<32>(a[s][0], a[s][1], a[s][2], a[s][3]); break;
      case 5: wait_stage<40>(a[s][0], a[s][1], a[s][2], a[s][3]); break;
      case 6: wait_stage<48>(a[s][0], a[s][1], a[s][2], a[s][3]); break;
    }
    bf16x8 w[4];
#pragma unroll
    for (int n = 0; n < 4; ++n) w[n] = cat8(lds_tr(c.tra[n] + s * 4096), lds_tr(c.tra[n] + s * 4096 + TR2));
    __builtin_amdgcn_sched_barrier(0);
    if (t + P < T) issue_w(c, t + P, s);
    __builtin_amdgcn_sched_barrier(0);
#pragma unroll
    for (int n = 0; n < 4; ++n)
#pragma unroll
      for (int m = 0; m < 4; ++m) {
        if (W_IS_A) acc[n][m] = __builtin_amdgcn_mfma_f32_16x16x32_bf16(w[n], a[s][m], acc[n][m], 0, 0, 0);
        else acc[m][n] = __builtin_amdgcn_mfma_f32_16x16x32_bf16(a[s][m], w[n], acc[m][n], 0, 0, 0);
      }
    __builtin_amdgcn_sched_barrier(0);
    if (t + P < T) issue_a(c, t + P, a[s][0], a[s][1], a[s][2], a[s][3]);
  }
}

constexpr int P1 = 6;
constexpr int K2_RING = P1 * 4096;
constexpr int K2_CONVK = 4 * K2_RING;
constexpr int K2_CONVU = K2_CONVK + 4 * 10240;
constexpr int K2_LDS = K2_CONVU + 2 * 4608;

__global__ __launch_bounds__(512) void k2_gemm1_conv(const u16* __restrict__ w1, const u16* __restrict__ b1,
                                                     const u16* __restrict__ Dskip, char* __restrict__ ws) {
  __shared__ __attribute__((aligned(16))) char smem[K2_LDS];
  const int tid = threadIdx.x, lane = tid & 63;
  const unsigned wv = rfl(tid >> 6);
  const unsigned sbase = (unsigned)(size_t)(LAS char*)smem;
  const int xcd = blockIdx.x & 7, slot = blockIdx.x >> 3;
  const int c16 = lane & 15, q = lane >> 4;
  if (wv < 4) {
    if (slot >= 28) return;
    const int dblk = (xcd & 1) * 4 + (slot & 3), fblk = (xcd >> 1) * 7 + (slot >> 2);
    GemmCtx c;
    c.srdA = __builtin_amdgcn_make_buffer_rsrc((void*)(ws + WS_HT), (short)0, D * L * 2, 0x00020000);
    c.srdW = __builtin_amdgcn_make_buffer_rsrc((void*)w1, (short)0, L * F * 2, 0x00020000);
    c.a_soff = (dblk * 64 + wv) * 4096;
    c.a_step = 4 * 4096;
    c.w_soff = wv * 32 * F * 2 + fblk * 128;
    c.w_step = 128 * F * 2;
    c.w_row8 = 8 * F * 2 - 1024;
    c.vA = lane * 16;
    {
      const int row = lane >> 3, ch = lane & 7;
      const int s0 = (row >> 1) & 1;
      c.vW0 = row * F * 2 + ((((ch >> 1) ^ s0) << 1) | (ch & 1)) * 16;
      c.vW1 = row * F * 2 + ((((ch >> 1) ^ (s0 | 2)) << 1) | (ch & 1)) * 16;
    }
    c.ring = sbase + wv * K2_RING;
    {
      const int row = 8 * q + (c16 >> 2);
      const int swz = ((row >> 1) & 1) | (((row >> 3) & 1) << 1);
#pragma unroll
      for (int n = 0; n < 4; ++n) c.tra[n] = c.ring + row * 128 + ((n ^ swz) << 5) + (lane & 3) * 8;
    }
    u32x2 bias[4];
#pragma unroll
    for (int ft = 0; ft < 4; ++ft) bias[ft] = *(const u32x2*)(b1 + fblk * 64 + ft * 16 + 4 * q);
    f32x4 acc[4][4];
#pragma unroll
    for (int a = 0; a < 4; ++a)
#pragma unroll
      for (int b = 0; b < 4; ++b) acc[a][b] = f32x4{0.f, 0.f, 0.f, 0.f};
    gemm_core<16, P1, true, 512>(c, acc);
    f32x4* mine = (f32x4*)(smem + wv * K2_RING);
#pragma unroll
    for (int dt = 0; dt < 4; ++dt)
#pragma unroll
      for (int ft = 0; ft < 4; ++ft) mine[(dt * 4 + ft) * 64 + lane] = acc[ft][dt];
    __syncthreads();
    f32x4 r[4];
#pragma unroll
    for (int ft = 0; ft < 4; ++ft) {
      r[ft] = f32x4{0.f, 0.f, 0.f, 0.f};
#pragma unroll
      for (int w = 0; w < 4; ++w) r[ft] += *(const f32x4*)(smem + w * K2_RING + ((wv * 4 + ft) * 64 + lane) * 16);
    }
    char* uT = ws + WS_UT;
#pragma unroll
    for (int kk = 0; kk < 2; ++kk) {
      u32x4 o;
#pragma unroll
      for (int h = 0; h < 2; ++h) {
        const int ft = 2 * kk + h;
        float g[4];
#pragma unroll
        for (int e = 0; e < 4; ++e) {
          const float bb = (e & 1) ? bfhi(bias[ft][e >> 1]) : bflo(bias[ft][e >> 1]);
          const float v = rbf(rbf(r[ft][e]) + bb);
          const float z = v * (0.7978845608028654f + 0.035677408136300125f * v * v);
          g[e] = v * __builtin_amdgcn_rcpf(1.f + __builtin_amdgcn_exp2f(-2.885390081777927f * z));
        }
        o[2 * h] = pk2(g[0], g[1]);
        o[2 * h + 1] = pk2(g[2], g[3]);
      }
      *(u32x4*)(uT + (size_t)((dblk * 56 + fblk * 2 + kk) * 4 + wv) * 1024 + lane * 16) = o;
    }
    return;
  }
  const unsigned cw = wv - 4, chl = cw >> 1, hf = cw & 1;
  const int d = blockIdx.x * 2 + chl;
  const unsigned kbase = sbase + K2_CONVK + cw * 10240;
  const unsigned ubase = sbase + K2_CONVU + chl * 4608;
  {
    __amdgpu_buffer_rsrc_t srdR = __builtin_amdgcn_make_buffer_rsrc((void*)(ws + WS_R), (short)0, D * RS * 2, 0x00020000);
    __amdgpu_buffer_rsrc_t srdU = __builtin_amdgcn_make_buffer_rsrc((void*)(ws + WS_XNT), (short)0, D * L * 2, 0x00020000);
    const unsigned rso = (d * RS + 8 + 1920 - 128 * hf) * 2;
#pragma unroll
    for (int j = 0; j < 10; ++j) {
      const int ci = j * 64 + lane, seg = ci / 20, cc = ci - seg * 20;
      const unsigned vo = (unsigned)((7 - (seg >> 2)) * 512 + cc * 16 + 8 - 2 * (seg & 3));
      asm volatile("s_mov_b32 m0, %0\n\ts_nop 0\n\tbuffer_load_dwordx4 %1, %2, %3 offen lds"
                   :: "s"(kbase + j * 1024), "v"(vo), "s"(srdR), "s"(rso - 7 * 512 - 8) : "memory");
    }
    const unsigned vo = lane * 16;
    asm volatile(
        "s_mov_b32 m0, %0\n\ts_nop 0\n\t"
        "buffer_load_dwordx4 %1, %2, %3 offen lds\n\t"
        "buffer_load_dwordx4 %1, %2, %3 offen offset:1024 lds\n\t"
        "buffer_load_dwordx4 %1, %2, %3 offen offset:2048 lds\n\t"
        "buffer_load_dwordx4 %1, %2, %3 offen offset:3072 lds"
        :: "s"(ubase + 512), "v"(vo), "s"(srdU), "s"((unsigned)(d * L * 2)) : "memory");
    if (lane < 32) *(LAS u32x4*)(size_t)(ubase + lane * 16) = u32x4{0u, 0u, 0u, 0u};
  }
  const float dsk = bf2f(Dskip[d]);
  WAITV(0);
  f32x4 acc[8];
#pragma unroll
  for (int a = 0; a < 8; ++a) acc[a] = f32x4{0.f, 0.f, 0.f, 0.f};
  const int sc = (c16 + 1) & 3;
  const unsigned aaddr = kbase + sc * 320 + (112 - (c16 + 1 - sc) + 8 * q) * 2;
  const unsigned baddr = ubase + 512 + (16 * (c16 - 8 * (int)hf - 1) + 8 * q) * 2;
#pragma unroll
  for (int dl = 0; dl < 4; ++dl) {
    bf16x8 af[8], bfr[8];
#pragma unroll
    for (int i = 0; i < 8; ++i) {
      const unsigned a = aaddr + i * 1280 - dl * 64;
      af[i] = cat8(*(const LAS s16x4*)(size_t)a, *(const LAS s16x4*)(size_t)(a + 8));
      bfr[i] = *(const LAS bf16x8*)(size_t)(baddr + i * 512 - dl * 64);
    }
#pragma unroll
    for (int nt = 0; nt < 8; ++nt)
#pragma unroll
      for (int i = 0; i <= nt; ++i) acc[nt] = __builtin_amdgcn_mfma_f32_16x16x32_bf16(af[i], bfr[nt - i], acc[nt], 0, 0, 0);
  }
  {
    f32x4* mine = (f32x4*)(smem + K2_CONVK + cw * 10240);
#pragma unroll
    for (int k = 0; k < 4; ++k) {
      const f32x4 lo = acc[k], hi = acc[4 + k];
      mine[k * 64 + lane] = hf ? lo : hi;
      acc[k] = hf ? hi : lo;
    }
  }
  __syncthreads();
  {
    const f32x4* other = (const f32x4*)(smem + K2_CONVK + (cw ^ 1) * 10240);
    u16* sT = (u16*)(ws + WS_ST) + (size_t)d * L;
#pragma unroll
    for (int k = 0; k < 4; ++k) {
      const f32x4 y = acc[k] + other[k * 64 + lane];
      const int l = 256 * (4 * hf + k) + 16 * c16 + 4 * q;
      const u32x2 uu = *(const LAS u32x2*)(size_t)(ubase + 512 + l * 2);
      const unsigned y01 = pk2(y[0], y[1]), y23 = pk2(y[2], y[3]);
      const unsigned k01 = pk2(bflo(uu[0]) * dsk, bfhi(uu[0]) * dsk), k23 = pk2(bflo(uu[1]) * dsk, bfhi(uu[1]) * dsk);
      u32x2 o;
      o[0] = pk2(bflo(y01) + bflo(k01), bfhi(y01) + bfhi(k01));
      o[1] = pk2(bflo(y23) + bflo(k23), bfhi(y23) + bfhi(k23));
      *(u32x2*)(sT + l) = o;
    }
  }
}

constexpr int P2 = 7;
constexpr int K3_RING = P2 * 4096;
constexpr int K3_S = 4 * K3_RING;
constexpr int K3_LDS = K3_S + 4 * 2048;

__global__ __launch_bounds__(256) void k3_gemm2(const u16* __restrict__ w2, const u16* __restrict__ b2,
                                                const u16* __restrict__ x, u16* __restrict__ out,
                                                char* __restrict__ ws) {
  __shared__ __attribute__((aligned(16))) char smem[K3_LDS];
  const int tid = threadIdx.x, lane = tid & 63;
  const unsigned wv = rfl(tid >> 6);
  const unsigned sbase = (unsigned)(size_t)(LAS char*)smem;
  const int xcd = blockIdx.x & 7, slot = blockIdx.x >> 3;
  const int c16 = lane & 15, q = lane >> 4;
  const int dblk = (xcd & 1) * 4 + (slot & 3), tblk = (xcd >> 1) * 8 + (slot >> 2);
  GemmCtx c;
  c.srdA = __builtin_amdgcn_make_buffer_rsrc((void*)(ws + WS_UT), (short)0, D * F * 2, 0x00020000);
  c.srdW = __builtin_amdgcn_make_buffer_rsrc((void*)w2, (short)0, L * F * 2, 0x00020000);
  c.a_soff = (dblk * 56 + wv) * 4096;
  c.a_step = 4 * 4096;
  c.w_soff = wv * 32 * L * 2 + tblk * 128;
  c.w_step = 128 * L * 2;
  c.w_row8 = 8 * L * 2 - 1024;
  c.vA = lane * 16;
  {
    const int row = lane >> 3, ch = lane & 7;
    c.vW0 = row * L * 2 + ((((ch >> 1) ^ ((row >> 1) & 3)) << 1) | (ch & 1)) * 16;
    c.vW1 = c.vW0;
  }
  c.ring = sbase + wv * K3_RING;
  {
    const int row = 4 * q + (c16 >> 2);
    const int swz = (row >> 1) & 3;
#pragma unroll
    for (int n = 0; n < 4; ++n) c.tra[n] = c.ring + row * 128 + ((n ^ swz) << 5) + (lane & 3) * 8;
  }
  const int tok = tblk * 64 + wv * 16 + c16;
  {
    __amdgpu_buffer_rsrc_t srdS = __builtin_amdgcn_make_buffer_rsrc((void*)(ws + WS_ST), (short)0, D * L * 2, 0x00020000);
    const unsigned vo = (lane >> 1) * L * 2 + (lane & 1) * 16;
    const unsigned so = (dblk * 64 * L + tblk * 64 + wv * 16) * 2;
    asm volatile(
        "s_mov_b32 m0, %0\n\ts_nop 0\n\t"
        "buffer_load_dwordx4 %1, %2, %3 offen lds\n\t"
        "buffer_load_dwordx4 %1, %2, %4 offen offset:1024 lds"
        :: "s"(sbase + K3_S + wv * 2048), "v"(vo), "s"(srdS), "s"(so), "s"(so + 32 * L * 2 - 1024) : "memory");
  }
  const u32x4 x0 = *(const u32x4*)(x + (size_t)tok * D + dblk * 64 + 8 * q);
  const u32x4 x1 = *(const u32x4*)(x + (size_t)tok * D + dblk * 64 + 32 + 8 * q);
  const float bb = bf2f(b2[tok]);
  f32x4 acc[4][4];
#pragma unroll
  for (int a = 0; a < 4; ++a)
#pragma unroll
    for (int b = 0; b < 4; ++b) acc[a][b] = f32x4{0.f, 0.f, 0.f, 0.f};
  gemm_core<14, P2, false, 2048>(c, acc);
  f32x4* mine = (f32x4*)(smem + wv * K3_RING);
#pragma unroll
  for (int tt = 0; tt < 4; ++tt)
#pragma unroll
    for (int dt = 0; dt < 4; ++dt) mine[(tt * 4 + dt) * 64 + lane] = acc[dt][tt];
  __syncthreads();
  const unsigned saddr = sbase + K3_S + wv * 2048 + (8 * q + (c16 >> 2)) * 32 + (lane & 3) * 8;
#pragma unroll
  for (int hh = 0; hh < 2; ++hh) {
    u32x4 o;
    const u32x4 xx = hh ? x1 : x0;
#pragma unroll
    for (int h = 0; h < 2; ++h) {
      const int dt = 2 * hh + h;
      f32x4 r = f32x4{0.f, 0.f, 0.f, 0.f};
#pragma unroll
      for (int w = 0; w < 4; ++w) r += *(const f32x4*)(smem + w * K3_RING + ((wv * 4 + dt) * 64 + lane) * 16);
      const s16x4 sv = lds_tr(saddr + (32 * hh + 4 * h) * 32);
      float res[4];
#pragma unroll
      for (int e = 0; e < 4; ++e) {
        const float v = rbf(rbf(r[e]) + bb);
        const float g = rbf(v * __builtin_amdgcn_rcpf(1.f + __builtin_amdgcn_exp2f(-1.44269504089f * v)));
        const float sg = rbf(bf2f((u16)sv[e]) * g);
        const unsigned xw = xx[2 * h + (e >> 1)];
        res[e] = ((e & 1) ? bfhi(xw) : bflo(xw)) + sg;
      }
      o[2 * h] = pk2(res[0], res[1]);
      o[2 * h + 1] = pk2(res[2], res[3]);
    }
    *(u32x4*)(out + (size_t)tok * D + dblk * 64 + 32 * hh + 8 * q) = o;
  }
}

extern "C" void kernel_launch(void* const* d_in, const int* in_sizes, int n_in, void* d_out, int out_size, void* d_ws,
                              size_t ws_size, hipStream_t stream) {
  const u16* x = (const u16*)d_in[0];
  char* ws = (char*)d_ws;
  k1_ln_kgen<<<LN_BLOCKS + D, 256, 0, stream>>>(x, (const u16*)d_in[1], (const u16*)d_in[2], (const u16*)d_in[3],
                                                (const u16*)d_in[4], (const u16*)d_in[5], (const u16*)d_in[6],
                                                (const u16*)d_in[7], ws);
  k2_gemm1_conv<<<256, 512, 0, stream>>>((const u16*)d_in[10], (const u16*)d_in[11], (const u16*)d_in[8], ws);
  k3_gemm2<<<256, 256, 0, stream>>>((const u16*)d_in[12], (const u16*)d_in[13], x, (u16*)d_out, ws);
}
```

```cpp
#include <hip/hip_runtime.h>
#include <stdint.h>

typedef unsigned short u16;
typedef __attribute__((ext_vector_type(8))) short bf16x8;
typedef __attribute__((ext_vector_type(4))) short s16x4;
typedef __attribute__((ext_vector_type(4))) float f32x4;
typedef __attribute__((ext_vector_type(2))) float f32x2;
typedef __attribute__((ext_vector_type(2))) __bf16 bf16x2;
typedef __attribute__((ext_vector_type(2))) _Float16 f16x2;
typedef __attribute__((ext_vector_type(8))) _Float16 f16x8;
typedef __attribute__((ext_vector_type(4))) unsigned u32x4;
typedef __attribute__((ext_vector_type(2))) unsigned u32x2;
typedef __amdgpu_buffer_rsrc_t rsrc_t;
#define LAS __attribute__((address_space(3)))
#define SB() __builtin_amdgcn_sched_barrier(0)

constexpr int L = 2048, D = 512, NS = 128, F = 1792;
constexpr int NE = 2;
constexpr int NH1 = 6, NH2 = 6;
constexpr int CODEB = 27648;

constexpr size_t WS_HT = 0;
constexpr size_t WS_XN = WS_HT + (size_t)D * L * 2;
constexpr size_t WS_UT = WS_XN + (size_t)D * L * 2;
constexpr size_t WS_ST = WS_UT + (size_t)D * F * 2;
constexpr size_t WS_CTL = WS_ST + (size_t)D * L * 2;
constexpr int CTL_L = 256;
constexpr int CTL_U = CTL_L + 8 * 8192;
constexpr int CTL_S = CTL_U + 32768;
constexpr int CTL_END = CTL_S + 32768;

constexpr int LN_ROWB = 1088;
constexpr int TB_ROWB = 272;
constexpr int TB_SZ = 57 * TB_ROWB;
constexpr int F_RING = 16384;
constexpr int F_X0 = 4 * F_RING;
constexpr int F_LN = F_X0 + 16384;
constexpr int F_TAB = F_LN + 16 * LN_ROWB;
constexpr int F_MISC = F_TAB + 4 * TB_SZ;
constexpr int F_BIAS = F_MISC + 128;
constexpr int F_B2 = F_BIAS + 128;
constexpr int F_DUMMY = F_B2 + 128;
constexpr int F_LDS = F_DUMMY + 1024;
constexpr int CV_U = 16640, CV_X = CV_U + 4608;
constexpr int F_ST = F_TAB, F_XT = F_TAB + 8192;
static_assert(CV_X + 8192 <= 2 * TB_SZ && CV_X >= TB_SZ + 3072 && F_LDS <= 160 * 1024 && F_MISC % 16 == 0, "lds map");
constexpr int M_HPH = 0, M_PUB1 = 36, M_GO1 = 40, M_CNT1 = 48, M_TAG = 56, M_FRONT = 64, M_GX = 80,
              M_CNT2 = 96, M_PUB2 = 100, M_GO2 = 104, M_CNT3 = 108, M_SRDY = 112;
constexpr unsigned SPIN_CAP = 1u << 18;
constexpr int AUX_SC1 = 16;

__device__ __forceinline__ rsrc_t mk_rsrc(const void* p, int bytes) {
  return __builtin_amdgcn_make_buffer_rsrc((void*)p, (short)0, bytes, 0x00020000);
}
__device__ __forceinline__ float bflo(unsigned v) { return __uint_as_float(v << 16); }
__device__ __forceinline__ float bfhi(unsigned v) { return __uint_as_float(v & 0xffff0000u); }
__device__ __forceinline__ unsigned pk2(float a, float b) {
  f32x2 f = {a, b};
  return __builtin_bit_cast(unsigned, __builtin_convertvector(f, bf16x2));
}
__device__ __forceinline__ float rbf(float f) { return bflo(pk2(f, f)); }
__device__ __forceinline__ unsigned rfl(unsigned v) { return __builtin_amdgcn_readfirstlane(v); }

template <int CTRL>
__device__ __forceinline__ float dpp_get(float v) {
  return __int_as_float(__builtin_amdgcn_update_dpp(0, __float_as_int(v), CTRL, 0xf, 0xf, true));
}
__device__ __forceinline__ float wave_sum(float v) {
  v += dpp_get<0xB1>(v);
  v += dpp_get<0x4E>(v);
  v += dpp_get<0x141>(v);
  v += dpp_get<0x140>(v);
  auto a = __builtin_amdgcn_permlane16_swap(__float_as_uint(v), __float_as_uint(v), false, false);
  v = __uint_as_float(a[0]) + __uint_as_float(a[1]);
  auto b = __builtin_amdgcn_permlane32_swap(__float_as_uint(v), __float_as_uint(v), false, false);
  return __uint_as_float(b[0]) + __uint_as_float(b[1]);
}
__device__ __forceinline__ float wave_max(float v) {
  v = __builtin_fmaxf(v, dpp_get<0xB1>(v));
  v = __builtin_fmaxf(v, dpp_get<0x4E>(v));
  v = __builtin_fmaxf(v, dpp_get<0x141>(v));
  v = __builtin_fmaxf(v, dpp_get<0x140>(v));
  auto a = __builtin_amdgcn_permlane16_swap(__float_as_uint(v), __float_as_uint(v), false, false);
  v = __builtin_fmaxf(__uint_as_float(a[0]), __uint_as_float(a[1]));
  auto b = __builtin_amdgcn_permlane32_swap(__float_as_uint(v), __float_as_uint(v), false, false);
  return __builtin_fmaxf(__uint_as_float(b[0]), __uint_as_float(b[1]));
}
__device__ __forceinline__ s16x4 lds_tr(unsigned addr) {
  return __builtin_amdgcn_ds_read_tr16_b64_v4i16((LAS s16x4*)(size_t)addr);
}
__device__ __forceinline__ bf16x8 cat8(s16x4 a, s16x4 b) { return __builtin_shufflevector(a, b, 0, 1, 2, 3, 4, 5, 6, 7); }

__device__ __forceinline__ void lds_wait(unsigned addr) {
  while (*(volatile LAS unsigned*)(size_t)addr == 0u) __builtin_amdgcn_s_sleep(1);
  asm volatile("" ::: "memory");
}
__device__ __forceinline__ void lds_wait4(unsigned addr) {
  for (;;) {
    const u32x4 f = *(volatile LAS u32x4*)(size_t)addr;
    if (f[0] & f[1] & f[2] & f[3]) break;
    __builtin_amdgcn_s_sleep(1);
  }
  asm volatile("" ::: "memory");
}
__device__ __forceinline__ void lds_set(unsigned addr, int lane) {
  asm volatile("s_waitcnt lgkmcnt(0)" ::: "memory");
  if (lane == 0) *(volatile LAS unsigned*)(size_t)addr = 1u;
}
__device__ __forceinline__ void hsync(unsigned addr, unsigned cw, int lane, unsigned ph) {
  asm volatile("s_waitcnt lgkmcnt(0)" ::: "memory");
  if (lane == 0) *(volatile LAS unsigned*)(size_t)(addr + cw * 4) = ph;
  for (;;) {
    const u32x4 f = *(volatile LAS u32x4*)(size_t)addr;
    if (min(min(f[0], f[1]), min(f[2], f[3])) >= ph) break;
    __builtin_amdgcn_s_sleep(1);
  }
  asm volatile("" ::: "memory");
}
__device__ __forceinline__ bool drained_last(unsigned cnt, int lane) {
  asm volatile("s_waitcnt vmcnt(0)" ::: "memory");
  unsigned old = 0;
  if (lane == 0) old = __hip_atomic_fetch_add((LAS unsigned*)(size_t)cnt, 1u, __ATOMIC_RELAXED, __HIP_MEMORY_SCOPE_WORKGROUP);
  return rfl(old) == 3u;
}
template <int SLP>
__device__ __forceinline__ void poll_tag(rsrc_t srdC, unsigned off, u32x2 tag) {
#pragma unroll 1
  for (unsigned spins = 0; spins < SPIN_CAP; ++spins) {
    const u32x2 f = __builtin_amdgcn_raw_buffer_load_b64(srdC, off, 0, AUX_SC1);
    if (f[0] == tag[0] && f[1] == tag[1]) off = 0x7fffffffu;
    if (__all(off == 0x7fffffffu)) break;
    __builtin_amdgcn_s_sleep(SLP);
  }
  __builtin_amdgcn_fence(__ATOMIC_ACQUIRE, "wavefront");
}

typedef f32x2 cf;
__device__ __forceinline__ cf cmul(cf a, cf b) {
  cf t = a.xx * b, r;
  asm("v_pk_fma_f32 %0, %1, %2, %3 op_sel:[1,1,0] op_sel_hi:[1,0,1] neg_lo:[0,1,0]" : "=v"(r) : "v"(a), "v"(b), "v"(t));
  return r;
}
__device__ __forceinline__ cf csq(cf a) { return cmul(a, a); }
__device__ __forceinline__ cf cexp_pos(float ar2, float wh, float wl, float p) {
  const float e = __builtin_amdgcn_exp2f(ar2 * p);
  const float th = wh * p;
  float tl = __builtin_fmaf(wh, p, -th);
  tl = __builtin_fmaf(wl, p, tl);
  const float r = __builtin_amdgcn_fractf(th) + tl;
  cf cs = {__builtin_amdgcn_cosf(r), __builtin_amdgcn_sinf(r)};
  return cs * e;
}

__device__ __forceinline__ void kgen_front(unsigned tbase, int lane, unsigned ldt, unsigned iar, unsigned iai,
                                           unsigned ibr, unsigned ibi, unsigned icr, unsigned ici) {
  const int c16 = lane & 15, q = lane >> 4;
  const float dt = rbf(expf(bflo(ldt)));
  const float ar2 = dt * bflo(iar) * 1.44269504089f, ai = dt * bflo(iai);
  const float I2PH = 0.15915494f, I2PL = 6.4206382e-9f;
  const float wh = ai * I2PH;
  const float wl = __builtin_fmaf(ai, I2PL, __builtin_fmaf(ai, I2PH, -wh));
#define PUT(row, z) *(LAS unsigned*)(size_t)(tbase + (row) * TB_ROWB + lane * 4) = __builtin_bit_cast(unsigned, __builtin_convertvector((z), f16x2))
  const cf f1 = cexp_pos(ar2, wh, wl, 128.f);
  const cf f2 = csq(f1), f4 = csq(f2);
  const cf e1 = cexp_pos(ar2, wh, wl, 1.f);
  const cf e2 = csq(e1), e3 = cmul(e2, e1), e4 = csq(e2), e5 = cmul(e4, e1), e6 = csq(e3), e7 = cmul(e4, e3);
  const cf f3 = cmul(f2, f1), f5 = cmul(f4, f1), f6 = csq(f3), f7 = cmul(f4, f3);
  PUT(0, (cf{1.f, 0.f})); PUT(1, e1); PUT(2, e2); PUT(3, e3); PUT(4, e4); PUT(5, e5); PUT(6, e6); PUT(7, e7);
  PUT(8, f1); PUT(9, cmul(f1, e1)); PUT(10, cmul(f1, e2)); PUT(11, cmul(f1, e3)); PUT(12, cmul(f1, e4));
  PUT(13, cmul(f1, e5)); PUT(14, cmul(f1, e6)); PUT(15, cmul(f1, e7));
  PUT(16, f2); PUT(17, cmul(f2, e2)); PUT(18, cmul(f2, e4)); PUT(19, cmul(f2, e6));
  PUT(20, f3); PUT(21, cmul(f3, e2)); PUT(22, cmul(f3, e4)); PUT(23, cmul(f3, e6));
  PUT(24, f4); PUT(25, cmul(f4, e4)); PUT(26, f5); PUT(27, cmul(f5, e4));
  PUT(28, f6); PUT(29, cmul(f6, e4)); PUT(30, f7); PUT(31, cmul(f7, e4));
  const cf c = cmul(cf{bflo(icr), bflo(ici)}, cf{dt * bflo(ibr), dt * bflo(ibi)});
  const int ex = __builtin_amdgcn_frexp_expf(wave_max(__builtin_fmaxf(__builtin_fabsf(c.x), __builtin_fabsf(c.y)))) - 12;
  const cf h0 = cexp_pos(ar2, wh, wl, 1024.f);
  const cf h1 = cmul(h0, f1), h2 = cmul(h0, f2), h3 = cmul(h2, f1);
  PUT(32, h0); PUT(33, h1); PUT(34, h2); PUT(35, h3);
  PUT(36, cmul(h0, f4)); PUT(37, cmul(h1, f4)); PUT(38, cmul(h2, f4)); PUT(39, cmul(h3, f4)); PUT(40, csq(h0));
  cf y1 = cexp_pos(ar2, wh, wl, 8.f);
  y1.y = -y1.y;
  const cf y2 = csq(y1), y4 = csq(y2), y8 = csq(y4);
  cf g[8];
  g[0] = cf{__builtin_amdgcn_ldexpf(c.x, -ex), -__builtin_amdgcn_ldexpf(c.y, -ex)};
  g[1] = cmul(g[0], y1);
  g[2] = cmul(g[0], y2);
  g[3] = cmul(g[1], y2);
#pragma unroll
  for (int b = 0; b < 4; ++b) g[4 + b] = cmul(g[b], y4);
#pragma unroll
  for (int b = 0; b < 8; ++b) { PUT(41 + b, g[b]); PUT(49 + b, cmul(g[b], y8)); }
#undef PUT
  f32x4 ka[3];
  f16x8 fb[4];
#pragma unroll
  for (int ks = 0; ks < 4; ++ks) fb[ks] = *(const LAS f16x8*)(size_t)(tbase + (41 + c16) * TB_ROWB + ks * 64 + q * 16);
#pragma unroll
  for (int m = 0; m < 3; ++m) {
    ka[m] = f32x4{0.f, 0.f, 0.f, 0.f};
#pragma unroll
    for (int ks = 0; ks < 4; ++ks)
      ka[m] = __builtin_amdgcn_mfma_f32_16x16x32_f16(*(const LAS f16x8*)(size_t)(tbase + (m * 16 + c16) * TB_ROWB + ks * 64 + q * 16),
                                                     fb[ks], ka[m], 0, 0, 0);
  }
#pragma unroll
  for (int m = 0; m < 3; ++m)
    *(LAS f32x4*)(size_t)(tbase + m * 1024 + lane * 16) =
        f32x4{__builtin_amdgcn_ldexpf(ka[m][0], ex), __builtin_amdgcn_ldexpf(ka[m][1], ex),
              __builtin_amdgcn_ldexpf(ka[m][2], ex), __builtin_amdgcn_ldexpf(ka[m][3], ex)};
}

struct GemmCtx {
  rsrc_t srdA, srdW;
  unsigned a_soff, w_soff, w_step, w_row8;
  unsigned vA, vW0, vW1;
  unsigned ring;
  unsigned rot;
  unsigned tra[4];
};
template <int T>
__device__ __forceinline__ unsigned wrap(unsigned v) { return T == 16 ? (v & 15u) : (v >= (unsigned)T ? v - T : v); }

template <int G>
__device__ __forceinline__ void issue_wnt(const GemmCtx& c, unsigned so, unsigned lds) {
  asm volatile("s_mov_b32 m0, %0\n\ts_nop 0\n\tbuffer_load_dwordx4 %1, %2, %3 offen offset:%4 nt lds"
               :: "s"(lds), "v"((G & 1) ? c.vW1 : c.vW0), "s"(c.srdW), "s"(so + G * c.w_row8), "n"(G * 1024) : "memory");
}
template <int G>
__device__ __forceinline__ void issue_w(const GemmCtx& c, unsigned so, unsigned lds) {
  asm volatile("s_mov_b32 m0, %0\n\ts_nop 0\n\tbuffer_load_dwordx4 %1, %2, %3 offen offset:%4 lds"
               :: "s"(lds), "v"((G & 1) ? c.vW1 : c.vW0), "s"(c.srdW), "s"(so + G * c.w_row8), "n"(G * 1024) : "memory");
}
template <int T>
__device__ __forceinline__ void issue_w4(const GemmCtx& c, int s, unsigned lds) {
  const unsigned so = c.w_soff + wrap<T>(s + c.rot) * c.w_step;
  issue_w<0>(c, so, lds); issue_w<1>(c, so, lds); issue_w<2>(c, so, lds); issue_w<3>(c, so, lds);
}
template <int G>
__device__ __forceinline__ void issue_a(const GemmCtx& c, unsigned so, bf16x8& a) {
  asm volatile("buffer_load_dwordx4 %0, %1, %2, %3 offen offset:%4 sc1"
               : "=&v"(a) : "v"(c.vA), "s"(c.srdA), "s"(so), "n"(G * 1024) : "memory");
}
template <int TR2>
__device__ __forceinline__ void read_w(const GemmCtx& c, unsigned off, bf16x8 (&w)[4]) {
#pragma unroll
  for (int n = 0; n < 4; ++n) w[n] = cat8(lds_tr(c.tra[n] + off), lds_tr(c.tra[n] + off + TR2));
}
__device__ __forceinline__ void lds_done(bf16x8 (&w)[4]) {
  asm volatile("s_waitcnt lgkmcnt(0)" : "+v"(w[0]), "+v"(w[1]), "+v"(w[2]), "+v"(w[3]) :: "memory");
}
constexpr bool from_held(int t, int H) { return t < 2 * H && (t & 1); }
constexpr int ring_idx(int t, int H) { return t < 2 * H ? t / 2 : t - H; }
constexpr int stage_of(int t, int H) { return from_held(t, H) ? t / 2 : H + ring_idx(t, H); }
constexpr int step_cnt(int j, int T, int H, bool NXT) {
  return j < 0 ? 4 : (j + 3 < T ? 4 : 0) + (j < 2 * (T - H - 4) ? 2 : 0) + (NXT && (j == T - 4 || j == T - 3) ? 4 : 0);
}

template <int T, int H, bool W_IS_A, int TR2, bool NXT, int t>
__device__ __forceinline__ void gemm_step(const GemmCtx& c, const GemmCtx& c2, unsigned xslot, f32x4 (&acc)[4][4],
                                          bf16x8 (&a)[4][4], bf16x8 (&held)[H][4], bf16x8 (&w)[2][4]) {
  static_assert(T - H - 4 <= H && 2 * (T - H - 4) <= T - 4, "schedule");
  constexpr int ab = t & 3, an = (t + 3) & 3;
  asm volatile("s_waitcnt vmcnt(%4)" : "+v"(a[ab][0]), "+v"(a[ab][1]), "+v"(a[ab][2]), "+v"(a[ab][3])
               : "n"(step_cnt(t - 2, T, H, NXT) + step_cnt(t - 1, T, H, NXT) + (NXT && t == T - 1 ? 4 : 0)) : "memory");
  constexpr bool rd = t + 1 < T && !from_held(t + 1, H);
  constexpr int rn = ring_idx(t + 1, H);
  if constexpr (rd) read_w<TR2>(c, ((H + rn) & 3) * 4096, w[rn & 1]);
  SB();
  constexpr bool iw = t < 2 * (T - H - 4), ia = t + 3 < T, in = NXT && (t == T - 4 || t == T - 3);
  constexpr int sw = H + t / 2 + 4, g0 = 2 * (t & 1);
  const unsigned wso = in ? c2.w_soff + wrap<14>(t + 4 - T + c2.rot) * c2.w_step : c.w_soff + wrap<T>((iw ? sw : 0) + c.rot) * c.w_step;
  const unsigned aso = c.a_soff + wrap<T>((ia ? stage_of(t + 3, H) : 0) + c.rot) * 16384;
#define WF(n) (from_held(t, H) ? held[from_held(t, H) ? t / 2 : 0][n] : w[ring_idx(t, H) & 1][n])
#define MM(n, m)                                                                                          \
  if (W_IS_A) acc[n][m] = __builtin_amdgcn_mfma_f32_16x16x32_bf16(WF(n), a[ab][m], acc[n][m], 0, 0, 0);  \
  else acc[m][n] = __builtin_amdgcn_mfma_f32_16x16x32_bf16(a[ab][m], WF(n), acc[m][n], 0, 0, 0);
#define IN(g) if constexpr (in) issue_w<g>(c2, wso, xslot + (t + 4 - T) * 16384);
  if constexpr (ia) issue_a<0>(c, aso, a[an][0]); SB();
  MM(0, 0) MM(0, 1) SB(); if constexpr (ia) issue_a<1>(c, aso, a[an][1]); SB();
  MM(0, 2) MM(0, 3) SB(); if constexpr (ia) issue_a<2>(c, aso, a[an][2]); SB();
  MM(1, 0) MM(1, 1) SB(); if constexpr (ia) issue_a<3>(c, aso, a[an][3]); SB();
  MM(1, 2) MM(1, 3) SB(); IN(0) SB();
  MM(2, 0) MM(2, 1) SB(); IN(1) if constexpr (iw) issue_w<g0>(c, wso, c.ring + (sw & 3) * 4096); SB();
  MM(2, 2) MM(2, 3) SB(); IN(2) SB();
  MM(3, 0) MM(3, 1) SB(); IN(3) if constexpr (iw) issue_w<g0 + 1>(c, wso, c.ring + (sw & 3) * 4096); SB();
  MM(3, 2) MM(3, 3) SB();
#undef MM
#undef WF
#undef IN
  if constexpr (rd) lds_done(w[rn & 1]);
  if constexpr (t + 1 < T) gemm_step<T, H, W_IS_A, TR2, NXT, t + 1>(c, c2, xslot, acc, a, held, w);
}

template <int T, int H, bool W_IS_A, int TR2, bool NXT>
__device__ __forceinline__ void gemm_run(const GemmCtx& c, const GemmCtx& c2, unsigned xslot, f32x4 (&acc)[4][4],
                                         bf16x8 (&held)[H][4]) {
  bf16x8 a[4][4], w[2][4];
#pragma unroll
  for (int s = 0; s < 3; ++s) {
    const unsigned so = c.a_soff + wrap<T>(stage_of(s, H) + c.rot) * 16384;
    issue_a<0>(c, so, a[s][0]); issue_a<1>(c, so, a[s][1]); issue_a<2>(c, so, a[s][2]); issue_a<3>(c, so, a[s][3]);
  }
  asm volatile("s_waitcnt vmcnt(12)" ::: "memory");
  read_w<TR2>(c, (H & 3) * 4096, w[0]);
  lds_done(w[0]);
#pragma unroll
  for (int i = 0; i < 4; ++i)
#pragma unroll
    for (int j = 0; j < 4; ++j) acc[i][j] = f32x4{0.f, 0.f, 0.f, 0.f};
  gemm_step<T, H, W_IS_A, TR2, NXT, 0>(c, c2, xslot, acc, a, held, w);
}

__device__ __forceinline__ f32x4 sum4(const char* src) {
  return (*(const f32x4*)(src) + *(const f32x4*)(src + F_RING)) + (*(const f32x4*)(src + 2 * F_RING) + *(const f32x4*)(src + 3 * F_RING));
}

__global__ __launch_bounds__(512, 2) void fused_kernel(const u16* __restrict__ x, const u16* __restrict__ Are,
                                                       const u16* __restrict__ Aim, const u16* __restrict__ Bre,
                                                       const u16* __restrict__ Bim, const u16* __restrict__ Cre,
                                                       const u16* __restrict__ Cim, const u16* __restrict__ logdt,
                                                       const u16* __restrict__ Dskip, const u16* __restrict__ w1,
                                                       const u16* __restrict__ b1, const u16* __restrict__ w2,
                                                       const u16* __restrict__ b2, u16* __restrict__ out,
                                                       char* __restrict__ ws) {
  __shared__ __attribute__((aligned(16))) char smem[F_LDS];
  const unsigned long long pc0 = __builtin_amdgcn_s_getpc() & ~127ull;
  const int tid = threadIdx.x, lane = tid & 63;
  const unsigned wv = rfl(tid >> 6);
  const unsigned sbase = (unsigned)(size_t)(LAS char*)smem;
  const int xcd = blockIdx.x & 7, slot = blockIdx.x >> 3;
  asm volatile("s_cmp_ge_u32 %4, 4\n\ts_cbranch_scc1 1f\n\t"
               "s_mov_b32 m0, %0\n\ts_nop 0\n\tbuffer_load_dword %1, %2, %3 offen lds\n1:"
               :: "s"(sbase + F_DUMMY + (wv & 3) * 256), "v"(lane * 128u), "s"(mk_rsrc((const void*)pc0, CODEB)),
                  "s"(wv * 8192), "s"(slot * 8 + wv) : "memory", "scc");
  asm volatile("" :: "s"(x), "s"(Are), "s"(Aim), "s"(Bre), "s"(Bim), "s"(Cre), "s"(Cim), "s"(logdt), "s"(Dskip), "s"(w1),
               "s"(b1), "s"(ws));
  const int oct = xcd * 32 + slot;
  const int c16 = lane & 15, q = lane >> 4;
  const bool has_g1 = xcd < 4 || slot < 24;
  const int dblk = slot & 7;
  const int fblk = xcd < 4 ? xcd * 4 + (slot >> 3) : 16 + (xcd - 4) * 3 + (has_g1 ? (slot >> 3) : 0);
  const int spare = (xcd - 4) * 8 + (slot - 24);
  const int tblk = xcd * 4 + (slot >> 3);
  const unsigned misc = sbase + F_MISC;
  const rsrc_t srdC = mk_rsrc(ws + WS_CTL, CTL_END);
  if (__builtin_expect(wv >= 4, 1)) {
    const unsigned cw = wv - 4, chl = cw >> 1, hf = cw & 1;
    const int d = oct * 2 + chl;
    __builtin_amdgcn_s_setprio(3);
    const rsrc_t srdH = mk_rsrc(ws + WS_HT, 2 * D * L * 2);
    if (cw == 0 && lane < 32) *(LAS unsigned*)(size_t)(misc + lane * 4) = 0u;
    u32x4 xr[2];
    u32x2 ep;
    unsigned dski, bq;
    asm volatile(
        "global_load_dwordx4 %0, %5, %9\n\t"
        "global_load_dwordx4 %1, %5, %9 offset:1024\n\t"
        "global_load_dwordx2 %2, %6, %10 sc1\n\t"
        "global_load_ushort %3, %7, %11\n\t"
        "global_load_dword %4, %8, %12"
        : "=&v"(xr[0]), "=&v"(xr[1]), "=&v"(ep), "=&v"(dski), "=&v"(bq)
        : "v"((oct * 8 + cw * 2) * (D * 2) + lane * 16), "v"(0), "v"(d * 2), "v"(fblk * 128 + (lane & 31) * 4), "s"(x),
          "s"(ws + WS_CTL), "s"(Dskip), "s"(b1)
        : "memory");
    asm volatile("s_waitcnt lgkmcnt(0)" ::: "memory");
    __builtin_amdgcn_s_barrier();
    SB();
    const unsigned kbase = sbase + F_TAB + chl * 2 * TB_SZ;
    const unsigned ubase = kbase + CV_U;
    const unsigned xch = kbase + CV_X;
    unsigned ga[16];
#pragma unroll
    for (int e = 0; e < 8; ++e) {
      const int l0 = 2047 - 1024 * (int)hf - 512 * (e >> 2) - 8 * lane - 2 * (e & 3);
      const unsigned rp = pk2((float)l0, (float)(l0 - 1));
      ga[2 * e] = xch + 2 * (unsigned)bflo(rp);
      ga[2 * e + 1] = xch + 2 * (unsigned)bfhi(rp);
      asm volatile("" : "+v"(ga[2 * e]), "+v"(ga[2 * e + 1]));
    }
    asm volatile("s_waitcnt vmcnt(3)" : "+v"(xr[0]), "+v"(xr[1]) :: "memory");
    {
      f32x2 v[2][4];
#pragma unroll
      for (int r = 0; r < 2; ++r)
#pragma unroll
        for (int e = 0; e < 4; ++e) v[r][e] = f32x2{bflo(xr[r][e]), bfhi(xr[r][e])};
#pragma unroll
      for (int pass = 0; pass < 2; ++pass) {
        float mean[2], var[2];
#pragma unroll
        for (int r = 0; r < 2; ++r) {
          const f32x2 sm = (v[r][0] + v[r][1]) + (v[r][2] + v[r][3]);
          mean[r] = wave_sum(sm.x + sm.y) * (1.f / D);
        }
#pragma unroll
        for (int r = 0; r < 2; ++r) {
          const f32x2 c0 = v[r][0] - mean[r], c1 = v[r][1] - mean[r], c2 = v[r][2] - mean[r], c3 = v[r][3] - mean[r];
          const f32x2 sq = __builtin_elementwise_fma(c0, c0, c1 * c1) + __builtin_elementwise_fma(c2, c2, c3 * c3);
          var[r] = wave_sum(sq.x + sq.y) * (1.f / D);
        }
#pragma unroll
        for (int r = 0; r < 2; ++r) {
          const float mb = rbf(mean[r]);
          const float den = rbf(__builtin_amdgcn_sqrtf(rbf(rbf(var[r]) + 1e-5f)));
          const float inv = __builtin_amdgcn_rcpf(den);
          u32x4 o;
#pragma unroll
          for (int e = 0; e < 4; ++e) {
            const f32x2 dd = v[r][e] - mb;
            const unsigned c = pk2(dd.x, dd.y);
            const f32x2 nn = f32x2{bflo(c), bfhi(c)} * inv;
            o[e] = pk2(nn.x, nn.y);
            v[r][e] = f32x2{bflo(o[e]), bfhi(o[e])};
          }
          *(u32x4*)(smem + F_LN + (pass * 8 + cw * 2 + r) * LN_ROWB + lane * 16) = o;
        }
      }
    }
    asm volatile("s_waitcnt vmcnt(0)" : "+v"(ep), "+v"(dski), "+v"(bq) :: "memory");
    hsync(misc + M_HPH, cw, lane, 1);
    {
      const int rr = c16 >> 2, p = lane & 3;
#pragma unroll
      for (int j = 0; j < 2; ++j) {
        const unsigned db = cw * 2 + j;
        const unsigned a = sbase + F_LN + (8 + rr) * LN_ROWB + db * 128 + (32 * (q >> 1) + 8 * p + 4 * (q & 1)) * 2;
        const bf16x8 val = cat8(lds_tr(a), lds_tr(a + 4 * LN_ROWB));
        __builtin_amdgcn_raw_buffer_store_b128(__builtin_bit_cast(u32x4, val), srdH,
                                               ((db * 64 + (oct >> 2)) * 4 + q) * 1024 + ((oct & 3) * 16 + c16) * 16, 0, AUX_SC1);
      }
#pragma unroll
      for (int j = 0; j < 2; ++j) {
        const unsigned db = cw * 2 + j;
        const unsigned a = sbase + F_LN + rr * LN_ROWB + db * 128 + (16 * q + 4 * p) * 2;
        const bf16x8 val = cat8(lds_tr(a), lds_tr(a + 4 * LN_ROWB));
        __builtin_amdgcn_raw_buffer_store_b128(__builtin_bit_cast(u32x4, val), srdH, D * L * 2 + oct * 8192 + (db * 64 + lane) * 16, 0, AUX_SC1);
      }
    }
    const unsigned long long tg = (((unsigned long long)ep[1] << 32) | ep[0]) + 0x9E3779B97F4A7C15ull;
    const u32x2 tag = {(unsigned)tg, (unsigned)(tg >> 32)};
    if (cw == 0) {
      if (lane == 0) *(volatile LAS u32x2*)(size_t)(misc + M_TAG) = tag;
      if (lane < 32) *(LAS unsigned*)(size_t)(sbase + F_BIAS + lane * 4) = bq;
      asm volatile("s_waitcnt lgkmcnt(0)" ::: "memory");
    }
    if (drained_last(misc + M_CNT1, lane)) {
      if (lane < 8) __builtin_amdgcn_raw_buffer_store_b64(tag, srdC, CTL_L + lane * 8192 + oct * 32, 0, AUX_SC1);
      lds_set(misc + M_PUB1, lane);
    }
    lds_wait4(misc + M_FRONT);
    {
      unsigned kp[3];
#pragma unroll
      for (int m = 0; m < 3; ++m) {
        const unsigned pa = kbase + m * 1024 + lane * 16 + hf * 8;
        const f32x2 t = *(const LAS f32x2*)(size_t)pa + *(const LAS f32x2*)(size_t)(pa + TB_SZ);
        kp[m] = pk2(t.x, t.y);
      }
      const int q1 = q & 1, q2 = q >> 1;
      const unsigned pa0 = xch + (4 * q1 + 128 * q2 + 8 * c16 + 2 * hf) * 2;
      const unsigned pa1 = xch + ((q2 ? 512 + 256 * q1 : 256 + 128 * q1) + 8 * c16) * 2 + hf * (q2 ? 256 : 8);
      const unsigned pa2 = xch + (1024 + 512 * q + 256 * hf + 8 * c16) * 2;
      const unsigned lim = xch + 2175 * 2;
      *(LAS unsigned*)(size_t)pa0 = kp[0];
      *(LAS u16*)(size_t)pa1 = (u16)kp[1];
      *(LAS u16*)(size_t)(pa1 + (q2 ? 8 : 4)) = (u16)(kp[1] >> 16);
      *(LAS u16*)(size_t)min(pa2, lim) = (u16)kp[2];
      *(LAS u16*)(size_t)min(pa2 + 256, lim) = (u16)(kp[2] >> 16);
    }
    u32x4 uq[2];
    hsync(misc + M_HPH, cw, lane, 2);
    {
      if (hf == 0 && lane < 32) *(LAS u32x4*)(size_t)(ubase + lane * 16) = u32x4{0u, 0u, 0u, 0u};
      u32x4 rq[2];
#pragma unroll
      for (int e = 0; e < 8; ++e)
        rq[e >> 2][e & 3] = (unsigned)*(const LAS u16*)(size_t)ga[2 * e] | ((unsigned)*(const LAS u16*)(size_t)ga[2 * e + 1] << 16);
      unsigned p0 = *(const LAS u16*)(size_t)(xch + 2048);
      p0 |= p0 << 16;
      unsigned p1 = p0;
      asm volatile("" : "+v"(rq[0]), "+v"(rq[1]), "+v"(p0));
      poll_tag<8>(srdC, CTL_L + xcd * 8192 + (cw * 64 + lane) * 32, tag);
#pragma unroll
      for (int j = 0; j < 2; ++j)
        uq[j] = __builtin_amdgcn_raw_buffer_load_b128(srdH, D * L * 2 + (cw * 64 + j * 32 + (lane >> 1)) * 8192 + oct * 32 + (lane & 1) * 16, 0, AUX_SC1);
      hsync(misc + M_HPH, cw, lane, 3);
      if (lane == 0) *(volatile LAS unsigned*)(size_t)(misc + M_GO1) = 1u;
      if (blockIdx.x == 0 && cw == 0 && lane == 0) __builtin_amdgcn_raw_buffer_store_b64(tag, srdC, 0, 0, AUX_SC1);
#pragma unroll
      for (int r = 0; r < 3; ++r) {
        const u32x4 cur = r < 2 ? rq[r < 2 ? r : 0] : u32x4{0u, 0u, 0u, 0u};
        const unsigned d0 = __builtin_amdgcn_update_dpp(p0, cur[2], 0x138, 0xf, 0xf, false);
        const unsigned d1 = __builtin_amdgcn_update_dpp(p1, cur[3], 0x138, 0xf, 0xf, false);
        p0 = __builtin_amdgcn_readlane(cur[2], 63);
        p1 = __builtin_amdgcn_readlane(cur[3], 63);
        const unsigned d2 = cur[0], d3 = cur[1], d4 = cur[2], d5 = cur[3];
        const unsigned a0 = __builtin_amdgcn_alignbit(d1, d0, 16), a1 = __builtin_amdgcn_alignbit(d2, d1, 16),
                       a2 = __builtin_amdgcn_alignbit(d3, d2, 16), a3 = __builtin_amdgcn_alignbit(d4, d3, 16),
                       a4 = __builtin_amdgcn_alignbit(d5, d4, 16);
        const unsigned dst = kbase + hf * 2048 + r * 1024 + lane * 16;
        if (r < 2 || (hf == 1 && lane < 4)) {
          *(LAS u32x4*)(size_t)(dst) = cur;
          *(LAS u32x4*)(size_t)(dst + 4160) = u32x4{a1, a2, a3, a4};
          *(LAS u32x4*)(size_t)(dst + 8320) = u32x4{d1, d2, d3, d4};
          *(LAS u32x4*)(size_t)(dst + 12480) = u32x4{a0, a1, a2, a3};
        }
      }
#pragma unroll
      for (int j = 0; j < 2; ++j)
        *(LAS u32x4*)(size_t)(sbase + F_TAB + (lane & 1) * 2 * TB_SZ + CV_U + 512 + (cw * 64 + j * 32 + (lane >> 1)) * 16) = uq[j];
    }
    hsync(misc + M_HPH, cw, lane, 4);
    __builtin_amdgcn_s_setprio(0);
    f32x4 acc[8];
#pragma unroll
    for (int a = 0; a < 8; ++a) acc[a] = f32x4{0.f, 0.f, 0.f, 0.f};
    {
      const int sc = (c16 + 1) & 3;
      const unsigned aaddr = kbase + sc * 4160 + (2032 - (c16 + 1 - sc) + 8 * q) * 2 - 7 * 512 - 3 * 64 - hf * 256;
      const unsigned baddr = ubase + 512 + (16 * (c16 - 1) + 8 * q) * 2 - 3 * 64 - hf * 256;
      bf16x8 af[2][8], bfr[2][8];
      auto ldfr = [&](int dl, int buf) {
#pragma unroll
        for (int i = 0; i < 8; ++i) {
          const unsigned a = aaddr + (7 - i) * 512 + (3 - dl) * 64;
          af[buf][i] = cat8(*(const volatile LAS s16x4*)(size_t)a, *(const volatile LAS s16x4*)(size_t)(a + 8));
          bfr[buf][i] = *(const LAS bf16x8*)(size_t)(baddr + i * 512 + (3 - dl) * 64);
        }
      };
      ldfr(0, 0);
#pragma unroll
      for (int dl = 0; dl < 4; ++dl) {
        SB();
        if (dl + 1 < 4) ldfr(dl + 1, (dl + 1) & 1);
#pragma unroll
        for (int i = 0; i < 8; ++i)
#pragma unroll
          for (int nt = i; nt < 8; ++nt)
            acc[nt] = __builtin_amdgcn_mfma_f32_16x16x32_bf16(af[dl & 1][i], bfr[dl & 1][nt - i], acc[nt], 0, 0, 0);
#pragma unroll
        for (int g = 0; g < 12; ++g) {
          __builtin_amdgcn_sched_group_barrier(0x8, 2, 0);
          __builtin_amdgcn_sched_group_barrier(0x100, 2, 0);
        }
        __builtin_amdgcn_sched_group_barrier(0x8, 12, 0);
        SB();
      }
    }
#pragma unroll
    for (int k = 0; k < 4; ++k) {
      const f32x4 lo = acc[k], hi = acc[4 + k];
      *(LAS f32x4*)(size_t)(xch + hf * 4096 + k * 1024 + lane * 16) = hf ? lo : hi;
      acc[k] = hf ? hi : lo;
    }
    hsync(misc + M_HPH, cw, lane, 5);
    const rsrc_t srdS = mk_rsrc(ws + WS_ST, D * L * 2);
    {
      const float dsk = bflo(dski);
#pragma unroll
      for (int p = 0; p < 2; ++p) {
        u32x2 o[2];
#pragma unroll
        for (int h = 0; h < 2; ++h) {
          const f32x4 y = acc[2 * p + h] + *(const LAS f32x4*)(size_t)(xch + (hf ^ 1) * 4096 + (2 * p + h) * 1024 + lane * 16);
          const u32x2 uu = *(const LAS u32x2*)(size_t)(ubase + 512 + hf * 2048 + (256 * (2 * p + h) + 16 * c16 + 4 * q) * 2);
#pragma unroll
          for (int e = 0; e < 2; ++e)
            o[h][e] = pk2(__builtin_fmaf(bflo(uu[e]), dsk, y[2 * e]), __builtin_fmaf(bfhi(uu[e]), dsk, y[2 * e + 1]));
        }
        const auto s0 = __builtin_amdgcn_permlane16_swap(o[0][0], o[1][0], false, false);
        const auto s1 = __builtin_amdgcn_permlane16_swap(o[0][1], o[1][1], false, false);
        __builtin_amdgcn_raw_buffer_store_b128(u32x4{s0[0], s1[0], s0[1], s1[1]}, srdS,
                                               d * (L * 2) + hf * 2048 + (256 * (2 * p + (q & 1)) + 16 * c16 + 8 * (q >> 1)) * 2, 0, AUX_SC1);
      }
    }
    if (drained_last(misc + M_CNT3, lane) && lane < 4)
      __builtin_amdgcn_raw_buffer_store_b64(tag, srdC, CTL_S + ((lane * 8 + xcd) * 32 + slot) * 32, 0, AUX_SC1);
    hsync(misc + M_HPH, cw, lane, 6);
    const int row = lane >> 3, ch = lane & 7;
    {
      const unsigned sox = ((tblk * 64 + cw * 16) * D + dblk * 64) * 2;
      asm volatile(
          "s_mov_b32 m0, %0\n\ts_nop 0\n\t"
          "buffer_load_dwordx4 %1, %2, %3 offen lds\n\t"
          "buffer_load_dwordx4 %1, %2, %4 offen offset:1024 lds"
          :: "s"(sbase + F_XT + cw * 2048), "v"(row * D * 2 + (ch ^ row) * 16), "s"(mk_rsrc(x, D * L * 2)), "s"(sox),
             "s"(sox + 8 * D * 2 - 1024) : "memory");
    }
    if (cw == 1) {
      lds_wait(misc + M_GO2);
      poll_tag<4>(srdC, CTL_S + (((xcd >> 1) * 8 + dblk) * 32 + (lane & 31)) * 32, tag);
      lds_set(misc + M_SRDY, lane);
    } else if (cw == 2) {
      if (lane < 32) *(unsigned*)(smem + F_B2 + lane * 4) = *(const unsigned*)(b2 + tblk * 64 + lane * 2);
    }
    lds_wait(misc + M_SRDY);
    {
      u32x4 sq[2];
#pragma unroll
      for (int g = 0; g < 2; ++g)
        sq[g] = __builtin_amdgcn_raw_buffer_load_b128(srdS, ((dblk * 64 + cw * 16 + g * 8 + row) * L + tblk * 64) * 2 + ch * 16, 0, AUX_SC1);
#pragma unroll
      for (int g = 0; g < 2; ++g)
        *(LAS u32x4*)(size_t)(sbase + F_ST + (cw * 16 + g * 8 + row) * 128 +
                              (((((ch >> 1) ^ (((row >> 1) & 1) | (g << 1))) << 1) | (ch & 1)) << 4)) = sq[g];
    }
    asm volatile("s_waitcnt vmcnt(0)" ::: "memory");
  } else {
    GemmCtx c;
    c.srdA = mk_rsrc(ws + WS_HT, D * L * 2);
    c.srdW = mk_rsrc(w1, L * F * 2);
    c.a_soff = (dblk * 64 + wv) * 4096;
    c.w_soff = wv * 32 * F * 2 + fblk * 128;
    c.w_step = 128 * F * 2;
    c.w_row8 = 8 * F * 2 - 1024;
    c.vA = lane * 16;
    {
      const int row = lane >> 3, ch = lane & 7, s0 = (row >> 1) & 1;
      c.vW0 = row * F * 2 + ((((ch >> 1) ^ s0) << 1) | (ch & 1)) * 16;
      c.vW1 = row * F * 2 + ((((ch >> 1) ^ (s0 | 2)) << 1) | (ch & 1)) * 16;
    }
    c.ring = sbase + wv * F_RING;
    c.rot = (2 * (dblk + xcd)) & 15;
    {
      const int row = 8 * q + (c16 >> 2);
      const int swz = ((row >> 1) & 1) | (((row >> 3) & 1) << 1);
#pragma unroll
      for (int n = 0; n < 4; ++n) c.tra[n] = c.ring + row * 128 + ((n ^ swz) << 5) + (lane & 3) * 8;
    }
    {
      const int d = oct * 2 + (wv >> 1);
      unsigned ldt, iar, iai, ibr, ibi, icr, ici;
      asm volatile(
          "global_load_ushort %0, %7, %9\n\t"
          "global_load_ushort %1, %8, %10\n\t"
          "global_load_ushort %2, %8, %11\n\t"
          "global_load_ushort %3, %8, %12\n\t"
          "global_load_ushort %4, %8, %13\n\t"
          "global_load_ushort %5, %8, %14\n\t"
          "global_load_ushort %6, %8, %15"
          : "=&v"(ldt), "=&v"(iar), "=&v"(iai), "=&v"(ibr), "=&v"(ibi), "=&v"(icr), "=&v"(ici)
          : "v"(d * 2), "v"((d * NS + (wv & 1) * 64 + lane) * 2), "s"(logdt), "s"(Are), "s"(Aim), "s"(Bre), "s"(Bim),
            "s"(Cre), "s"(Cim)
          : "memory");
      __builtin_amdgcn_s_barrier();
#pragma unroll
      for (int s = 0; s < NE; ++s) {
        const unsigned so = c.w_soff + wrap<16>(s + c.rot) * c.w_step, lds = c.ring + s * 4096;
        issue_wnt<0>(c, so, lds); issue_wnt<1>(c, so, lds); issue_wnt<2>(c, so, lds); issue_wnt<3>(c, so, lds);
      }
      asm volatile("s_waitcnt vmcnt(%7)" : "+v"(ldt), "+v"(iar), "+v"(iai), "+v"(ibr), "+v"(ibi), "+v"(icr), "+v"(ici)
                   : "n"(4 * NE) : "memory");
      kgen_front(sbase + F_TAB + wv * TB_SZ, lane, ldt, iar, iai, ibr, ibi, icr, ici);
      lds_set(misc + M_FRONT + wv * 4, lane);
      lds_wait(misc + M_PUB1);
    }
    GemmCtx c2;
    c2.srdA = mk_rsrc(ws + WS_UT, D * F * 2);
    c2.srdW = mk_rsrc(w2, L * F * 2);
    c2.a_soff = (dblk * 56 + wv) * 4096;
    c2.w_soff = wv * 32 * L * 2 + tblk * 128;
    c2.w_step = 128 * L * 2;
    c2.w_row8 = 8 * L * 2 - 1024;
    c2.vA = lane * 16;
    c2.vW0 = (lane >> 3) * L * 2 + (((((lane & 7) >> 1) ^ ((lane >> 4) & 3)) << 1) | (lane & 1)) * 16;
    c2.vW1 = c2.vW0;
    c2.ring = c.ring;
    c2.rot = (((dblk + xcd) & 7) * 14) >> 3;
    const unsigned xslot = sbase + F_X0 + wv * 4096;
    f32x4 acc[4][4];
    if (has_g1) {
      bf16x8 held[NH1][4];
#pragma unroll
      for (int s = NE; s < 4; ++s) issue_w4<16>(c, s, c.ring + s * 4096);
#pragma unroll
      for (int s = 0; s < NH1; ++s) {
        asm volatile("s_waitcnt vmcnt(12)" ::: "memory");
        read_w<512>(c, (s & 3) * 4096, held[s]);
        lds_done(held[s]);
        issue_w4<16>(c, s + 4, c.ring + (s & 3) * 4096);
      }
      lds_wait(misc + M_GO1);
      gemm_run<16, NH1, true, 512, true>(c, c2, xslot, acc, held);
      __builtin_amdgcn_s_setprio(3);
      f32x4* mine = (f32x4*)(smem + wv * F_RING);
#pragma unroll
      for (int dt = 0; dt < 4; ++dt)
#pragma unroll
        for (int ft = 0; ft < 4; ++ft) mine[(dt * 4 + ft) * 64 + lane] = acc[ft][dt];
      lds_set(misc + M_GX + wv * 4, lane);
      lds_wait4(misc + M_GX);
#pragma unroll
      for (int kk = 0; kk < 2; ++kk) {
        u32x4 o;
#pragma unroll
        for (int h = 0; h < 2; ++h) {
          const u32x2 bias = *(const u32x2*)(smem + F_BIAS + (32 * kk + 16 * h + 4 * q) * 2);
          const f32x4 r = sum4(smem + ((wv * 4 + 2 * kk + h) * 64 + lane) * 16);
#pragma unroll
          for (int e2 = 0; e2 < 2; ++e2) {
            const f32x2 v = f32x2{r[2 * e2], r[2 * e2 + 1]} + f32x2{bflo(bias[e2]), bfhi(bias[e2])};
            const f32x2 t = v * __builtin_elementwise_fma(v * v, f32x2{-0.10294324f, -0.10294324f}, f32x2{-2.3022082f, -2.3022082f});
            const f32x2 dn = f32x2{__builtin_amdgcn_exp2f(t.x), __builtin_amdgcn_exp2f(t.y)} + 1.f;
            const f32x2 g = v * f32x2{__builtin_amdgcn_rcpf(dn.x), __builtin_amdgcn_rcpf(dn.y)};
            o[2 * h + e2] = pk2(g.x, g.y);
          }
        }
        __builtin_amdgcn_raw_buffer_store_b128(o, c2.srdA, ((dblk * 56 + fblk * 2 + kk) * 4 + wv) * 1024 + lane * 16, 0, AUX_SC1);
      }
      if (drained_last(misc + M_CNT2, lane)) {
        const u32x2 tag = *(volatile LAS u32x2*)(size_t)(misc + M_TAG);
        if (lane < 4) __builtin_amdgcn_raw_buffer_store_b64(tag, srdC, CTL_U + ((lane * 8 + dblk) * 32 + fblk) * 32, 0, AUX_SC1);
        lds_set(misc + M_PUB2, lane);
      }
      lds_wait(misc + M_PUB2);
    } else {
      unsigned pfd = 0;
      for (unsigned g = spare * 4 + wv; g < F / 2; g += 128)
        asm volatile("buffer_load_dword %0, %1, %2, %3 offen\n\ts_waitcnt vmcnt(0)"
                     : "+v"(pfd) : "v"(lane * 128), "s"(c2.srdW), "s"(g * 8192) : "memory");
      issue_w4<14>(c2, 0, xslot);
      issue_w4<14>(c2, 1, xslot + 16384);
    }
    {
      const int row = 4 * q + (c16 >> 2);
      const int swz = (row >> 1) & 3;
#pragma unroll
      for (int n = 0; n < 4; ++n) c2.tra[n] = c2.ring + row * 128 + ((n ^ swz) << 5) + (lane & 3) * 8;
    }
    bf16x8 held2[NH2][4];
#pragma unroll
    for (int s = 2; s < 6; ++s) issue_w4<14>(c2, s, c2.ring + (s & 3) * 4096);
    asm volatile("s_waitcnt vmcnt(16)" ::: "memory");
    read_w<2048>(c2, xslot - c2.ring, held2[0]);
    read_w<2048>(c2, xslot - c2.ring + 16384, held2[1]);
#pragma unroll
    for (int s = 2; s < NH2; ++s) {
      asm volatile("s_waitcnt vmcnt(12)" ::: "memory");
      read_w<2048>(c2, (s & 3) * 4096, held2[s]);
      lds_done(held2[s]);
      issue_w4<14>(c2, s + 4, c2.ring + (s & 3) * 4096);
    }
    if (wv == 0) {
      const u32x2 tag = *(volatile LAS u32x2*)(size_t)(misc + M_TAG);
      poll_tag<4>(srdC, CTL_U + (((xcd >> 1) * 8 + dblk) * 32 + min(lane, 27)) * 32, tag);
      lds_set(misc + M_GO2, lane);
    }
    lds_wait(misc + M_GO2);
    gemm_run<14, NH2, false, 2048, false>(c2, c2, 0u, acc, held2);
    f32x4* mine = (f32x4*)(smem + wv * F_RING);
#pragma unroll
    for (int t = 0; t < 4; ++t)
#pragma unroll
      for (int dt = 0; dt < 4; ++dt) mine[(t * 4 + dt) * 64 + lane] = acc[dt][t];
  }
  __syncthreads();
  {
    const unsigned tt = wv & 3, hh = wv >> 2;
    const int srow = 32 * hh + 8 * q + (c16 >> 2);
    const unsigned saddr = sbase + F_ST + srow * 128 + ((tt ^ (((srow >> 1) & 1) | (((srow >> 3) & 1) << 1))) << 5) + (lane & 3) * 8;
    const int tok = tblk * 64 + tt * 16 + c16;
    const u32x4 xx = *(const u32x4*)(smem + F_XT + (tt * 16 + c16) * 128 + (((4 * hh + q) ^ (c16 & 7)) << 4));
    const float bb = bflo(*(const u16*)(smem + F_B2 + (tt * 16 + c16) * 2));
    u32x4 o;
#pragma unroll
    for (int h = 0; h < 2; ++h) {
      const f32x4 r = sum4(smem + ((tt * 4 + 2 * hh + h) * 64 + lane) * 16);
      const u32x2 sv = __builtin_bit_cast(u32x2, lds_tr(saddr + h * 512));
#pragma unroll
      for (int e2 = 0; e2 < 2; ++e2) {
        const f32x2 v = f32x2{r[2 * e2], r[2 * e2 + 1]} + bb;
        const f32x2 t = v * -1.44269504089f;
        const f32x2 dn = f32x2{__builtin_amdgcn_exp2f(t.x), __builtin_amdgcn_exp2f(t.y)} + 1.f;
        const f32x2 g = v * f32x2{__builtin_amdgcn_rcpf(dn.x), __builtin_amdgcn_rcpf(dn.y)};
        const unsigned xw = xx[2 * h + e2];
        const f32x2 res = __builtin_elementwise_fma(f32x2{bflo(sv[e2]), bfhi(sv[e2])}, g, f32x2{bflo(xw), bfhi(xw)});
        o[2 * h + e2] = pk2(res.x, res.y);
      }
    }
    *(u32x4*)(out + (size_t)tok * D + dblk * 64 + 32 * hh + 8 * q) = o;
  }
}

extern "C" void kernel_launch(void* const* d_in, const int* in_sizes, int n_in, void* d_out, int out_size, void* d_ws,
                              size_t ws_size, hipStream_t stream) {
  fused_kernel<<<256, 512, 0, stream>>>((const u16*)d_in[0], (const u16*)d_in[1], (const u16*)d_in[2], (const u16*)d_in[3],
                                        (const u16*)d_in[4], (const u16*)d_in[5], (const u16*)d_in[6], (const u16*)d_in[7],
                                        (const u16*)d_in[8], (const u16*)d_in[10], (const u16*)d_in[11],
                                        (const u16*)d_in[12], (const u16*)d_in[13], (u16*)d_out, (char*)d_ws);
}
```
